```python
import torch

def _softmax(x, axis=-1):
    return torch.softmax(x, dim=axis)
def _gelu(x):
    return torch.nn.functional.gelu(x, approximate="tanh")
def _silu(x):
    return torch.nn.functional.silu(x)
def _sigmoid(x):
    return torch.sigmoid(x)
def _layernorm(x, axis=-1, eps=1e-5):
    m = x.mean(dim=axis, keepdim=True)
    v = x.var(dim=axis, keepdim=True, unbiased=False)
    return (x - m) / torch.sqrt(v + eps)
def _scatter_add(tgt, idx, src, axis=0):
    return tgt.clone().index_add_(axis, idx, src)
def _segment_sum(src, seg, n):
    out = torch.zeros((n,) + src.shape[1:], dtype=src.dtype)
    return out.index_add_(0, seg, src)
def _pad(x, pw):
    flat = tuple(v for pair in reversed(pw) for v in pair)
    return torch.nn.functional.pad(x, flat)

kernel_name = 'sequential_preLN__sliding_block_cached__dense_activation_squared_relu__decode'

def setup_inputs():

    _w = (lambda a: (lambda t: t.to(torch.bfloat16) if t.is_floating_point() else t)(torch.as_tensor(a)))
    def _A_setup():
        B, Sq, D = 1, 1, 512
        Hq, Hkv, Sk, Dh = 32, 32, 32768, 128
        assert Hq % Hkv == 0, (Hq, Hkv)
        return {
            "x": _w((lambda *s: torch.randn(*s, dtype=torch.bfloat16) * 0.05)(B, Sq, D)),
            "k_cache": _w((lambda *s: torch.randn(*s, dtype=torch.bfloat16) * 3.2)(B, Hkv, Sk, Dh)),
            "v_cache": _w((lambda *s: torch.randn(*s, dtype=torch.bfloat16) * 0.4)(B, Hkv, Sk, Dh)),
            "wq": _w((lambda *s: torch.randn(*s, dtype=torch.bfloat16) * 3.2)(D, Hq * Dh) * (D ** -0.5)),
            "wo": _w((lambda *s: torch.randn(*s, dtype=torch.bfloat16) * 0.4)(Hq * Dh, D) * ((Hq * Dh) ** -0.5)),
        }
    _A_in = _A_setup()

    def _B_setup():
        B, S, D, F = 1, 1, 512, 1376
        return {
            "x": _w((lambda *s: torch.randn(*s, dtype=torch.bfloat16) * 0.05)(B, S, D)),
            "w1": _w((lambda *s: torch.randn(*s, dtype=torch.bfloat16) * 0.05)(D, F)),
            "b1": _w((lambda *s: torch.randn(*s, dtype=torch.bfloat16) * 0.8)(F)),
            "w2": _w((lambda *s: torch.randn(*s, dtype=torch.bfloat16) * 0.05)(F, D)),
            "b2": _w((lambda *s: torch.randn(*s, dtype=torch.bfloat16) * 3.2)(D)),
        }
    _B_in = _B_setup()

    return {
        "A_x": _A_in["x"],
        "A_k_cache": _A_in["k_cache"],
        "A_v_cache": _A_in["v_cache"],
        "A_wq": _A_in["wq"],
        "A_wo": _A_in["wo"],
        "B_x": _B_in["x"],
        "B_w1": _B_in["w1"],
        "B_b1": _B_in["b1"],
        "B_w2": _B_in["w2"],
        "B_b2": _B_in["b2"],
    }

def reference(A_x, A_k_cache, A_v_cache, A_wq, A_wo, B_x, B_w1, B_b1, B_w2, B_b2):
    def _A_ref(x, k_cache, v_cache, wq, wo):
        B, Sq, D = x.shape
        Hq, Dh = 32, 128
        Hkv, Sk = k_cache.shape[1], k_cache.shape[2]
        W = min(4096, max(256, Sk // 4))
        rep = Hq // Hkv
        q = (lambda x, *d: x.permute(*d))((x @ wq).reshape(B, Sq, Hq, Dh), 0, 2, 1, 3)
        k = (lambda x, n, axis: torch.repeat_interleave(x, n, dim=axis))(k_cache[:, :, Sk - W:, :], rep, axis=1)
        v = (lambda x, n, axis: torch.repeat_interleave(x, n, dim=axis))(v_cache[:, :, Sk - W:, :], rep, axis=1)
        scores = torch.einsum("bhqd,bhkd->bhqk", q, k) * (Dh ** -0.5)
        p = _softmax(scores, axis=-1)
        o = torch.einsum("bhqk,bhkd->bhqd", p, v)
        return (lambda x, *d: x.permute(*d))(o, 0, 2, 1, 3).reshape(B, Sq, Hq * Dh) @ wo

    def _B_ref(x, w1, b1, w2, b2):
        h = torch.einsum("bsd,df->bsf", x, w1) + b1
        zero = torch.zeros_like(h)
        r = torch.where(h > zero, h, zero)
        h = r * r
        out = torch.einsum("bsf,fd->bsd", h, w2) + b2
        return out

    x = A_x
    h = x + _A_ref(_layernorm(x, axis=-1), A_k_cache, A_v_cache, A_wq, A_wo)
    h2 = h + _B_ref(_layernorm(h, axis=-1), B_w1, B_b1, B_w2, B_b2)
    return h2
```

```cpp
#include <hip/hip_runtime.h>
#include <hip/hip_bf16.h>
#include <cstdint>

#define GAS __attribute__((address_space(1)))
typedef unsigned int u32;
typedef unsigned short u16;
typedef unsigned long long u64;
typedef u32 u32x4 __attribute__((ext_vector_type(4)));
typedef float f32x4 __attribute__((ext_vector_type(4)));
typedef __bf16 bf16x2 __attribute__((ext_vector_type(2)));
typedef GAS u32 gu32;
typedef GAS u64 gu64;
#define RLX_AGENT __ATOMIC_RELAXED, __HIP_MEMORY_SCOPE_AGENT

constexpr int D = 512, NH = 32, DH = 128, SK = 32768, WIN = 4096, F = 1376;
constexpr int NSPLIT = 8;
constexpr int PREC = 132;
constexpr int FG = 43, FC = 32;
constexpr int NRED = 32;
constexpr size_t WS_PART = 0;
constexpr size_t WS_PA = 256 * 1024;
constexpr size_t WS_HG = WS_PA + 32 * 512 * 8;
constexpr size_t WS_PF = WS_HG + 512 * 8;
constexpr size_t WS_TMO = WS_PF + (size_t)FG * 512 * 8;
constexpr size_t WS_ZERO_BYTES = WS_TMO + 256 - WS_PA;
constexpr unsigned kSpinCap = 1u << 18;

__device__ __forceinline__ float bf16r(float x) { return (float)(__bf16)x; }
__device__ __forceinline__ float bflo(u32 w) { return __uint_as_float(w << 16); }
__device__ __forceinline__ float bfhi(u32 w) { return __uint_as_float(w & 0xffff0000u); }
__device__ __forceinline__ float bf2f(u16 b) { return __uint_as_float(((u32)b) << 16); }
__device__ __forceinline__ u16 f2bf_bits(float x) { return __builtin_bit_cast(u16, (__bf16)x); }
__device__ __forceinline__ float dot2(u32 k, u32 q, float acc) {
  return __builtin_amdgcn_fdot2_f32_bf16(__builtin_bit_cast(bf16x2, k), __builtin_bit_cast(bf16x2, q), acc, false);
}
template <int CTRL> __device__ __forceinline__ float dpp(float x) {
  return __builtin_bit_cast(float, __builtin_amdgcn_mov_dpp(__builtin_bit_cast(int, x), CTRL, 0xf, 0xf, true));
}
constexpr int XOR1 = 0xB1, XOR2 = 0x4E, HMIR = 0x141, MIR = 0x140, ROR8 = 0x128;
__device__ __forceinline__ float row16_sum(float x) {
  x += dpp<XOR1>(x); x += dpp<XOR2>(x); x += dpp<HMIR>(x); x += dpp<MIR>(x); return x;
}
__device__ __forceinline__ float xrow_sum(float x) {
  auto s = __builtin_amdgcn_permlane16_swap(__float_as_uint(x), __float_as_uint(x), false, false);
  x = __uint_as_float(s[0]) + __uint_as_float(s[1]);
  auto t = __builtin_amdgcn_permlane32_swap(__float_as_uint(x), __float_as_uint(x), false, false);
  return __uint_as_float(t[0]) + __uint_as_float(t[1]);
}
__device__ __forceinline__ float xrow_max(float x) {
  auto s = __builtin_amdgcn_permlane16_swap(__float_as_uint(x), __float_as_uint(x), false, false);
  x = fmaxf(__uint_as_float(s[0]), __uint_as_float(s[1]));
  auto t = __builtin_amdgcn_permlane32_swap(__float_as_uint(x), __float_as_uint(x), false, false);
  return fmaxf(__uint_as_float(t[0]), __uint_as_float(t[1]));
}
__device__ __forceinline__ float wave_sum(float x) { return xrow_sum(row16_sum(x)); }

__device__ __forceinline__ bool spin_fail(unsigned& spins, gu32* tmo, int lane) {
  __builtin_amdgcn_s_sleep(1);
  if (++spins > kSpinCap) { if (lane == 0) __hip_atomic_store(tmo, 1u, RLX_AGENT); return true; }
  return (spins & 255u) == 0u && __builtin_amdgcn_readfirstlane(__hip_atomic_load(tmo, RLX_AGENT));
}
__device__ __forceinline__ void store_granule(gu64* g, float v) {
  __hip_atomic_store(g, (1ull << 32) | (u64)__float_as_uint(v), RLX_AGENT);
}

__global__ void __launch_bounds__(1024, 4) k1_attn(const u32x4* __restrict__ x, const u32x4* __restrict__ kc,
                                                    const u32x4* __restrict__ vc, const u32x4* __restrict__ wq,
                                                    float* __restrict__ part, u32x4* __restrict__ zero_area) {
  __shared__ __attribute__((aligned(16))) float lds[16 * 32 + 16 * 132 + 64 + 64];
  float* xs = lds;
  float* mrg = lds + 16 * 32;
  u16* qb = (u16*)(lds + 16 * 32 + 16 * 132);
  const int b = blockIdx.x, xcd = b & 7, idx = b >> 3, h = xcd + 8 * (idx >> 3), s = idx & 7;
  const int tid = threadIdx.x, lane = tid & 63, wave = __builtin_amdgcn_readfirstlane(tid >> 6), r = lane >> 4, c = lane & 15;

  const u32x4 xv = x[lane];
  u32x4 wqv[8];
#pragma unroll
  for (int i = 0; i < 8; ++i) wqv[i] = wq[((size_t)(32 * wave + 4 * i + r) * 4096 + h * 128 + c * 8) / 8];
  const size_t kbase = ((size_t)h * SK + (SK - WIN) + s * 512 + wave * 32) * (DH / 8);
  u32x4 kv[8], vv[8];
#pragma unroll
  for (int i = 0; i < 8; ++i) kv[i] = __builtin_nontemporal_load(kc + kbase + i * 64 + lane);
#pragma unroll
  for (int i = 0; i < 8; ++i) vv[i] = __builtin_nontemporal_load(vc + kbase + i * 64 + lane);
  {
    constexpr int NV = (int)(WS_ZERO_BYTES / 16);
    for (int u = b * 1024 + tid; u < NV; u += 256 * 1024) zero_area[u] = u32x4{0u, 0u, 0u, 0u};
  }
  float xf[8];
#pragma unroll
  for (int k = 0; k < 4; ++k) { xf[2 * k] = bflo(xv[k]); xf[2 * k + 1] = bfhi(xv[k]); }
  float s1 = 0.f;
#pragma unroll
  for (int j = 0; j < 8; ++j) s1 += xf[j];
  const float mean = wave_sum(s1) * (1.f / 512.f);
  float d2 = 0.f;
#pragma unroll
  for (int j = 0; j < 8; ++j) { const float d = xf[j] - mean; d2 += d * d; }
  const float var = wave_sum(d2) * (1.f / 512.f);
  const float mb = bf16r(mean), den = bf16r(sqrtf(bf16r(bf16r(var) + 1e-5f)));
  if ((lane >> 2) == wave) {
#pragma unroll
    for (int j = 0; j < 8; ++j) xs[wave * 32 + (lane & 3) * 8 + j] = bf16r(bf16r(xf[j] - mb) / den);
  }
  float qa[8];
#pragma unroll
  for (int j = 0; j < 8; ++j) qa[j] = 0.f;
#pragma unroll
  for (int i = 0; i < 8; ++i) {
    const float xr = xs[wave * 32 + 4 * i + r];
#pragma unroll
    for (int k = 0; k < 4; ++k) { qa[2 * k] = fmaf(xr, bflo(wqv[i][k]), qa[2 * k]); qa[2 * k + 1] = fmaf(xr, bfhi(wqv[i][k]), qa[2 * k + 1]); }
  }
#pragma unroll
  for (int j = 0; j < 8; ++j) qa[j] = xrow_sum(qa[j]);
  if (r == 0) {
    *(f32x4*)(mrg + wave * PREC + c * 8) = f32x4{qa[0], qa[1], qa[2], qa[3]};
    *(f32x4*)(mrg + wave * PREC + c * 8 + 4) = f32x4{qa[4], qa[5], qa[6], qa[7]};
  }
  __syncthreads();
  if (tid < 128) {
    float q = 0.f;
#pragma unroll
    for (int w = 0; w < 16; ++w) q += mrg[w * PREC + tid];
    qb[tid] = f2bf_bits(q);
  }
  __syncthreads();
  const u32x4 qpk = *(const u32x4*)(qb + c * 8);
  float sc[8];
#pragma unroll
  for (int i = 0; i < 8; ++i) {
    float d = 0.f;
#pragma unroll
    for (int k = 0; k < 4; ++k) d = dot2(kv[i][k], qpk[k], d);
    d = row16_sum(d);
    sc[i] = bf16r(bf16r(d) * 0.08838834764831845f);
  }
  float mw = sc[0];
#pragma unroll
  for (int i = 1; i < 8; ++i) mw = fmaxf(mw, sc[i]);
  mw = xrow_max(mw);
  float lw = 0.f, acc[8];
#pragma unroll
  for (int j = 0; j < 8; ++j) acc[j] = 0.f;
#pragma unroll
  for (int i = 0; i < 8; ++i) {
    const float p = __expf(sc[i] - mw);
    lw += p;
#pragma unroll
    for (int k = 0; k < 4; ++k) { acc[2 * k] = fmaf(p, bflo(vv[i][k]), acc[2 * k]); acc[2 * k + 1] = fmaf(p, bfhi(vv[i][k]), acc[2 * k + 1]); }
  }
  lw = xrow_sum(lw);
#pragma unroll
  for (int j = 0; j < 8; ++j) acc[j] = xrow_sum(acc[j]);
  if (r == 0) {
    *(f32x4*)(mrg + wave * PREC + c * 8) = f32x4{acc[0], acc[1], acc[2], acc[3]};
    *(f32x4*)(mrg + wave * PREC + c * 8 + 4) = f32x4{acc[4], acc[5], acc[6], acc[7]};
    if (c == 0) { mrg[wave * PREC + 128] = mw; mrg[wave * PREC + 129] = lw; }
  }
  __syncthreads();
  if (tid < 128) {
    float M = mrg[128];
#pragma unroll
    for (int w = 1; w < 16; ++w) M = fmaxf(M, mrg[w * PREC + 128]);
    float L = 0.f, o = 0.f;
#pragma unroll
    for (int w = 0; w < 16; ++w) {
      const float e = __expf(mrg[w * PREC + 128] - M);
      L = fmaf(e, mrg[w * PREC + 129], L);
      o = fmaf(e, mrg[w * PREC + tid], o);
    }
    float* rec = part + (size_t)(h * NSPLIT + s) * PREC;
    rec[tid] = o;
    if (tid == 0) { rec[128] = M; rec[129] = L; }
  }
}

__global__ void __launch_bounds__(256, 1) k2_tail(const u16* __restrict__ x, const float* __restrict__ part,
                                                   const u32x4* __restrict__ wo, const u32x4* __restrict__ w1,
                                                   const u16* __restrict__ b1, const u32x4* __restrict__ w2,
                                                   const u16* __restrict__ b2, u16* __restrict__ out, unsigned char* ws) {
  __shared__ __attribute__((aligned(16))) float lds[512 + 256 + 4 * 512];
  const int b = blockIdx.x, tid = threadIdx.x, lane = tid & 63, wave = __builtin_amdgcn_readfirstlane(tid >> 6);
  gu64* PA = (gu64*)(ws + WS_PA);
  gu64* HG = (gu64*)(ws + WS_HG);
  gu64* PF = (gu64*)(ws + WS_PF);
  gu32* tmo = (gu32*)(ws + WS_TMO);
  const int h = b >> 3, cs = b & 7;
  const bool is_ffn = (b >= NRED) && (b < NRED + FG);
  const int g = b - NRED;
  u32x4 wov[4];
#pragma unroll
  for (int i = 0; i < 4; ++i) wov[i] = wo[((size_t)(h * 128 + (wave * 4 + i) * 8 + (lane >> 3)) * 512 + cs * 64 + (lane & 7) * 8) / 8];
  u32x4 w1v[8], w2v[8];
  if (is_ffn) {
#pragma unroll
    for (int i = 0; i < 8; ++i) w1v[i] = w1[((size_t)((wave * 8 + i) * 16 + (lane >> 2)) * F + g * FC + (lane & 3) * 8) / 8];
#pragma unroll
    for (int i = 0; i < 8; ++i) w2v[i] = w2[((size_t)(g * FC + wave * 8 + i) * 512 + lane * 8) / 8];
  }
  float* oh = lds;
  if (tid < 128) {
    const float* rec = part + (size_t)h * NSPLIT * PREC;
    float M = rec[128];
#pragma unroll
    for (int sp = 1; sp < NSPLIT; ++sp) M = fmaxf(M, rec[sp * PREC + 128]);
    float L = 0.f, o = 0.f;
#pragma unroll
    for (int sp = 0; sp < NSPLIT; ++sp) {
      const float e = __expf(rec[sp * PREC + 128] - M);
      L = fmaf(e, rec[sp * PREC + 129], L);
      o = fmaf(e, rec[sp * PREC + tid], o);
    }
    oh[tid] = bf16r(o / L);
  }
  __syncthreads();
  {
    float acc[8];
#pragma unroll
    for (int j = 0; j < 8; ++j) acc[j] = 0.f;
#pragma unroll
    for (int i = 0; i < 4; ++i) {
      const float ov = oh[(wave * 4 + i) * 8 + (lane >> 3)];
#pragma unroll
      for (int k = 0; k < 4; ++k) { acc[2 * k] = fmaf(ov, bflo(wov[i][k]), acc[2 * k]); acc[2 * k + 1] = fmaf(ov, bfhi(wov[i][k]), acc[2 * k + 1]); }
    }
#pragma unroll
    for (int j = 0; j < 8; ++j) { acc[j] += dpp<ROR8>(acc[j]); acc[j] = xrow_sum(acc[j]); }
    float* red = lds + 512;
    if (lane < 8) {
      *(f32x4*)(red + wave * 64 + lane * 8) = f32x4{acc[0], acc[1], acc[2], acc[3]};
      *(f32x4*)(red + wave * 64 + lane * 8 + 4) = f32x4{acc[4], acc[5], acc[6], acc[7]};
    }
    __syncthreads();
    if (tid < 64) {
      const float v = (red[tid] + red[64 + tid]) + (red[128 + tid] + red[192 + tid]);
      store_granule(PA + h * 512 + cs * 64 + tid, v);
    }
  }
  float hval = 0.f;
  if (b < NRED && wave == 0) {
    const int col = b * 16 + (lane & 15);
    float v[8];
    for (unsigned spins = 0;;) {
      bool ok = true;
#pragma unroll
      for (int k = 0; k < 8; ++k) {
        const u64 gv = __hip_atomic_load(PA + ((lane >> 4) + 4 * k) * 512 + col, RLX_AGENT);
        v[k] = __uint_as_float((u32)gv); ok &= (gv >> 32) == 1ull;
      }
      if (__all(ok)) break;
      if (spin_fail(spins, tmo, lane)) break;
    }
    float a = 0.f;
#pragma unroll
    for (int k = 0; k < 8; ++k) a += v[k];
    a = xrow_sum(a);
    hval = bf16r(bf2f(x[col]) + bf16r(a));
    if (lane < 16) store_granule(HG + col, hval);
  }
  if (is_ffn) {
    float* hn = lds;
    if (wave == 0) {
      float v[8];
      for (unsigned spins = 0;;) {
        bool ok = true;
#pragma unroll
        for (int k = 0; k < 8; ++k) {
          const u64 gv = __hip_atomic_load(HG + k * 64 + lane, RLX_AGENT);
          v[k] = __uint_as_float((u32)gv); ok &= (gv >> 32) == 1ull;
        }
        if (__all(ok)) break;
        if (spin_fail(spins, tmo, lane)) break;
      }
      float s1 = 0.f;
#pragma unroll
      for (int k = 0; k < 8; ++k) s1 += v[k];
      const float mean = wave_sum(s1) * (1.f / 512.f);
      float d2 = 0.f;
#pragma unroll
      for (int k = 0; k < 8; ++k) { const float d = v[k] - mean; d2 += d * d; }
      const float var = wave_sum(d2) * (1.f / 512.f);
      const float mb = bf16r(mean), den = bf16r(sqrtf(bf16r(bf16r(var) + 1e-5f)));
#pragma unroll
      for (int k = 0; k < 8; ++k) hn[k * 64 + lane] = bf16r(bf16r(v[k] - mb) / den);
    }
    __syncthreads();
    float acc[8];
#pragma unroll
    for (int j = 0; j < 8; ++j) acc[j] = 0.f;
#pragma unroll
    for (int i = 0; i < 8; ++i) {
      const float hv = hn[(wave * 8 + i) * 16 + (lane >> 2)];
#pragma unroll
      for (int k = 0; k < 4; ++k) { acc[2 * k] = fmaf(hv, bflo(w1v[i][k]), acc[2 * k]); acc[2 * k + 1] = fmaf(hv, bfhi(w1v[i][k]), acc[2 * k + 1]); }
    }
#pragma unroll
    for (int j = 0; j < 8; ++j) {
      float t = acc[j];
      t += __shfl_xor(t, 4); t += __shfl_xor(t, 8); t = xrow_sum(t);
      acc[j] = t;
    }
    float* red = lds + 512;
    if (lane < 4) {
      *(f32x4*)(red + wave * 32 + lane * 8) = f32x4{acc[0], acc[1], acc[2], acc[3]};
      *(f32x4*)(red + wave * 32 + lane * 8 + 4) = f32x4{acc[4], acc[5], acc[6], acc[7]};
    }
    __syncthreads();
    float* act = lds + 512 + 128;
    if (tid < 32) {
      const float t = bf16r((red[tid] + red[32 + tid]) + (red[64 + tid] + red[96 + tid]));
      const float h1 = bf16r(t + bf2f(b1[g * FC + tid]));
      const float rr = fmaxf(h1, 0.f);
      act[tid] = bf16r(rr * rr);
    }
    __syncthreads();
#pragma unroll
    for (int j = 0; j < 8; ++j) acc[j] = 0.f;
#pragma unroll
    for (int i = 0; i < 8; ++i) {
      const float av = act[wave * 8 + i];
#pragma unroll
      for (int k = 0; k < 4; ++k) { acc[2 * k] = fmaf(av, bflo(w2v[i][k]), acc[2 * k]); acc[2 * k + 1] = fmaf(av, bfhi(w2v[i][k]), acc[2 * k + 1]); }
    }
    float* red2 = lds + 512 + 256;
    *(f32x4*)(red2 + wave * 512 + lane * 8) = f32x4{acc[0], acc[1], acc[2], acc[3]};
    *(f32x4*)(red2 + wave * 512 + lane * 8 + 4) = f32x4{acc[4], acc[5], acc[6], acc[7]};
    __syncthreads();
#pragma unroll
    for (int u = 0; u < 2; ++u) {
      const int col = u * 256 + tid;
      const float v = (red2[col] + red2[512 + col]) + (red2[1024 + col] + red2[1536 + col]);
      store_granule(PF + g * 512 + col, v);
    }
  }
  if (b < NRED && wave == 0) {
    const int col = b * 16 + (lane & 15);
    float v[11];
    for (unsigned spins = 0;;) {
      bool ok = true;
#pragma unroll
      for (int k = 0; k < 11; ++k) {
        const int gg = (lane >> 4) + 4 * k;
        if (gg < FG) {
          const u64 gv = __hip_atomic_load(PF + gg * 512 + col, RLX_AGENT);
          v[k] = __uint_as_float((u32)gv); ok &= (gv >> 32) == 1ull;
        } else v[k] = 0.f;
      }
      if (__all(ok)) break;
      if (spin_fail(spins, tmo, lane)) break;
    }
    float a = 0.f;
#pragma unroll
    for (int k = 0; k < 11; ++k) a += v[k];
    a = xrow_sum(a);
    const float o = bf16r(hval + bf16r(bf16r(a) + bf2f(b2[col])));
    if (lane < 16) out[col] = f2bf_bits(o);
  }
}

extern "C" void kernel_launch(void* const* d_in, const int* in_sizes, int n_in,
                              void* d_out, int out_size, void* d_ws, size_t ws_size,
                              hipStream_t stream) {
  const u32x4* x = (const u32x4*)d_in[0];
  const u32x4* kc = (const u32x4*)d_in[1];
  const u32x4* vc = (const u32x4*)d_in[2];
  const u32x4* wq = (const u32x4*)d_in[3];
  const u32x4* wo = (const u32x4*)d_in[4];
  const u32x4* w1 = (const u32x4*)d_in[6];
  const u16* b1 = (const u16*)d_in[7];
  const u32x4* w2 = (const u32x4*)d_in[8];
  const u16* b2 = (const u16*)d_in[9];
  unsigned char* ws = (unsigned char*)d_ws;
  hipLaunchKernelGGL(k1_attn, dim3(256), dim3(1024), 0, stream, x, kc, vc, wq, (float*)(ws + WS_PART), (u32x4*)(ws + WS_PA));
  hipLaunchKernelGGL(k2_tail, dim3(256), dim3(256), 0, stream, (const u16*)x, (const float*)(ws + WS_PART), wo, w1, b1, w2, b2,
                     (u16*)d_out, ws);
}
```

```cpp
#include <hip/hip_runtime.h>
#include <hip/hip_bf16.h>
#include <cstdint>

#define GAS __attribute__((address_space(1)))
typedef unsigned int u32;
typedef unsigned short u16;
typedef unsigned long long u64;
typedef u32 u32x4 __attribute__((ext_vector_type(4)));
typedef float f32x4 __attribute__((ext_vector_type(4)));
typedef __bf16 bf16x2 __attribute__((ext_vector_type(2)));
typedef GAS u32 gu32;
typedef GAS u64 gu64;
#define RLX_AGENT __ATOMIC_RELAXED, __HIP_MEMORY_SCOPE_AGENT

constexpr int D = 512, NH = 32, DH = 128, SK = 32768, WIN = 4096, F = 1376;
constexpr int NSPLIT = 8;
#define DEPTH 2
constexpr int NCH = 4;
constexpr int PREC = 132;
constexpr int FG = 43, FC = 32;
constexpr int NRED = 32;
constexpr size_t WS_PART = 0;
constexpr size_t WS_PA = 256 * 1024;
constexpr size_t WS_HG = WS_PA + 32 * 512 * 8;
constexpr size_t WS_PF = WS_HG + 512 * 8;
constexpr size_t WS_TMO = WS_PF + (size_t)FG * 512 * 8;
constexpr size_t WS_ZERO_BYTES = WS_TMO + 256 - WS_PA;
constexpr unsigned kSpinCap = 1u << 18;
#define STAMP(ws_, kern, i) do {} while (0)

__device__ __forceinline__ float bf16r(float x) { return (float)(__bf16)x; }
__device__ __forceinline__ float bflo(u32 w) { return __uint_as_float(w << 16); }
__device__ __forceinline__ float bfhi(u32 w) { return __uint_as_float(w & 0xffff0000u); }
__device__ __forceinline__ float bf2f(u16 b) { return __uint_as_float(((u32)b) << 16); }
__device__ __forceinline__ u16 f2bf_bits(float x) { return __builtin_bit_cast(u16, (__bf16)x); }
__device__ __forceinline__ float dot2(u32 k, u32 q, float acc) {
  return __builtin_amdgcn_fdot2_f32_bf16(__builtin_bit_cast(bf16x2, k), __builtin_bit_cast(bf16x2, q), acc, false);
}
template <int CTRL> __device__ __forceinline__ float dpp(float x) {
  return __builtin_bit_cast(float, __builtin_amdgcn_mov_dpp(__builtin_bit_cast(int, x), CTRL, 0xf, 0xf, true));
}
constexpr int XOR1 = 0xB1, XOR2 = 0x4E, HMIR = 0x141, MIR = 0x140, ROR8 = 0x128;
__device__ __forceinline__ float row16_sum(float x) {
  x += dpp<XOR1>(x); x += dpp<XOR2>(x); x += dpp<HMIR>(x); x += dpp<MIR>(x); return x;
}
__device__ __forceinline__ float xrow_sum(float x) {
  auto s = __builtin_amdgcn_permlane16_swap(__float_as_uint(x), __float_as_uint(x), false, false);
  x = __uint_as_float(s[0]) + __uint_as_float(s[1]);
  auto t = __builtin_amdgcn_permlane32_swap(__float_as_uint(x), __float_as_uint(x), false, false);
  return __uint_as_float(t[0]) + __uint_as_float(t[1]);
}
__device__ __forceinline__ float xrow_max(float x) {
  auto s = __builtin_amdgcn_permlane16_swap(__float_as_uint(x), __float_as_uint(x), false, false);
  x = fmaxf(__uint_as_float(s[0]), __uint_as_float(s[1]));
  auto t = __builtin_amdgcn_permlane32_swap(__float_as_uint(x), __float_as_uint(x), false, false);
  return fmaxf(__uint_as_float(t[0]), __uint_as_float(t[1]));
}
__device__ __forceinline__ float wave_sum(float x) { return xrow_sum(row16_sum(x)); }

__device__ __forceinline__ bool spin_fail(unsigned& spins, gu32* tmo, int lane) {
  __builtin_amdgcn_s_sleep(1);
  if (++spins > kSpinCap) { if (lane == 0) __hip_atomic_store(tmo, 1u, RLX_AGENT); return true; }
  return (spins & 255u) == 0u && __builtin_amdgcn_readfirstlane(__hip_atomic_load(tmo, RLX_AGENT));
}
__device__ __forceinline__ void store_granule(gu64* g, float v) {
  __hip_atomic_store(g, (1ull << 32) | (u64)__float_as_uint(v), RLX_AGENT);
}

__global__ void __launch_bounds__(1024, 4) k1_attn(const u32x4* __restrict__ x, const u32x4* __restrict__ kc,
                                                    const u32x4* __restrict__ vc, const u32x4* __restrict__ wq,
                                                    float* __restrict__ part, u32x4* __restrict__ zero_area) {
  __shared__ __attribute__((aligned(16))) float lds[16 * 32 + 16 * 132 + 64 + 64];
  float* xs = lds;
  float* mrg = lds + 16 * 32;
  u16* qb = (u16*)(lds + 16 * 32 + 16 * 132);
  const int b = blockIdx.x, xcd = b & 7, idx = b >> 3, h = xcd + 8 * (idx >> 3), s = idx & 7;
  const int tid = threadIdx.x, lane = tid & 63, wave = __builtin_amdgcn_readfirstlane(tid >> 6), r = lane >> 4, c = lane & 15;

  STAMP(part, 0, 0);
  const u32x4 xv = x[lane];
  u32x4 wqv[8];
#pragma unroll
  for (int i = 0; i < 8; ++i) wqv[i] = __builtin_nontemporal_load(wq + ((size_t)(32 * wave + 4 * i + r) * 4096 + h * 128 + c * 8) / 8);
  const size_t kbase = ((size_t)h * SK + (SK - WIN) + s * 512 + wave * 32) * (DH / 8) + lane;
  u32x4 kv[NCH][2], vv[NCH][2];
#define ISSUE(ch) do { kv[ch][0] = __builtin_nontemporal_load(kc + kbase + (ch) * 128); kv[ch][1] = __builtin_nontemporal_load(kc + kbase + (ch) * 128 + 64); \
                       vv[ch][0] = __builtin_nontemporal_load(vc + kbase + (ch) * 128); vv[ch][1] = __builtin_nontemporal_load(vc + kbase + (ch) * 128 + 64); } while (0)
#pragma unroll
  for (int ch = 0; ch < DEPTH; ++ch) ISSUE(ch);
  {
    constexpr int NV = (int)(WS_ZERO_BYTES / 16);
    for (int u = b * 1024 + tid; u < NV; u += 256 * 1024) zero_area[u] = u32x4{0u, 0u, 0u, 0u};
  }
  float xf[8];
#pragma unroll
  for (int k = 0; k < 4; ++k) { xf[2 * k] = bflo(xv[k]); xf[2 * k + 1] = bfhi(xv[k]); }
  float s1 = 0.f;
#pragma unroll
  for (int j = 0; j < 8; ++j) s1 += xf[j];
  const float mean = wave_sum(s1) * (1.f / 512.f);
  float d2 = 0.f;
#pragma unroll
  for (int j = 0; j < 8; ++j) { const float d = xf[j] - mean; d2 += d * d; }
  const float var = wave_sum(d2) * (1.f / 512.f);
  const float mb = bf16r(mean), den = bf16r(sqrtf(bf16r(bf16r(var) + 1e-5f)));
  if ((lane >> 2) == wave) {
#pragma unroll
    for (int j = 0; j < 8; ++j) xs[wave * 32 + (lane & 3) * 8 + j] = bf16r(bf16r(xf[j] - mb) / den);
  }
  STAMP(part, 0, 1);
  float qa[8];
#pragma unroll
  for (int j = 0; j < 8; ++j) qa[j] = 0.f;
#pragma unroll
  for (int i = 0; i < 8; ++i) {
    const float xr = xs[wave * 32 + 4 * i + r];
#pragma unroll
    for (int k = 0; k < 4; ++k) { qa[2 * k] = fmaf(xr, bflo(wqv[i][k]), qa[2 * k]); qa[2 * k + 1] = fmaf(xr, bfhi(wqv[i][k]), qa[2 * k + 1]); }
  }
#pragma unroll
  for (int j = 0; j < 8; ++j) qa[j] = xrow_sum(qa[j]);
  if (r == 0) {
    *(f32x4*)(mrg + wave * PREC + c * 8) = f32x4{qa[0], qa[1], qa[2], qa[3]};
    *(f32x4*)(mrg + wave * PREC + c * 8 + 4) = f32x4{qa[4], qa[5], qa[6], qa[7]};
  }
  __syncthreads();
  if (tid < 128) {
    float q = 0.f;
#pragma unroll
    for (int w = 0; w < 16; ++w) q += mrg[w * PREC + tid];
    qb[tid] = f2bf_bits(q);
  }
  __syncthreads();
  const u32x4 qpk = *(const u32x4*)(qb + c * 8);
  STAMP(part, 0, 2);
  float mw = -INFINITY, lw = 0.f, acc[8];
#pragma unroll
  for (int j = 0; j < 8; ++j) acc[j] = 0.f;
#pragma unroll
  for (int ch = 0; ch < NCH; ++ch) {
    float sc[2];
#pragma unroll
    for (int i = 0; i < 2; ++i) {
      float d = 0.f;
#pragma unroll
      for (int k = 0; k < 4; ++k) d = dot2(kv[ch][i][k], qpk[k], d);
      d = row16_sum(d);
      sc[i] = bf16r(bf16r(d) * 0.08838834764831845f);
    }
    const float mn = fmaxf(mw, xrow_max(fmaxf(sc[0], sc[1])));
    const float alpha = __expf(mw - mn);
    const float p0 = __expf(sc[0] - mn), p1 = __expf(sc[1] - mn);
    mw = mn;
    lw = fmaf(lw, alpha, p0 + p1);
#pragma unroll
    for (int k = 0; k < 4; ++k) {
      acc[2 * k] = fmaf(p1, bflo(vv[ch][1][k]), fmaf(p0, bflo(vv[ch][0][k]), acc[2 * k] * alpha));
      acc[2 * k + 1] = fmaf(p1, bfhi(vv[ch][1][k]), fmaf(p0, bfhi(vv[ch][0][k]), acc[2 * k + 1] * alpha));
    }
    if (ch + DEPTH < NCH) ISSUE(ch + DEPTH);
  }
#undef ISSUE
  STAMP(part, 0, 4);
  lw = xrow_sum(lw);
#pragma unroll
  for (int j = 0; j < 8; ++j) acc[j] = xrow_sum(acc[j]);
  if (r == 0) {
    *(f32x4*)(mrg + wave * PREC + c * 8) = f32x4{acc[0], acc[1], acc[2], acc[3]};
    *(f32x4*)(mrg + wave * PREC + c * 8 + 4) = f32x4{acc[4], acc[5], acc[6], acc[7]};
    if (c == 0) { mrg[wave * PREC + 128] = mw; mrg[wave * PREC + 129] = lw; }
  }
  __syncthreads();
  if (tid < 128) {
    float M = mrg[128];
#pragma unroll
    for (int w = 1; w < 16; ++w) M = fmaxf(M, mrg[w * PREC + 128]);
    float L = 0.f, o = 0.f;
#pragma unroll
    for (int w = 0; w < 16; ++w) {
      const float e = __expf(mrg[w * PREC + 128] - M);
      L = fmaf(e, mrg[w * PREC + 129], L);
      o = fmaf(e, mrg[w * PREC + tid], o);
    }
    float* rec = part + (size_t)(h * NSPLIT + s) * PREC;
    rec[tid] = o;
    STAMP(part, 0, 5);
    if (tid == 0) { rec[128] = M; rec[129] = L; }
  }
}

constexpr int K2_GRID = 256;
__global__ void __launch_bounds__(256, 1) k2_tail(const u16* __restrict__ x, const float* __restrict__ part,
                                                   const u32x4* __restrict__ wo, const u32x4* __restrict__ w1,
                                                   const u16* __restrict__ b1, const u32x4* __restrict__ w2,
                                                   const u16* __restrict__ b2, u16* __restrict__ out, unsigned char* ws) {
  __shared__ __attribute__((aligned(16))) float lds[4 * 512 + 4 * 512 + 128];
  const int tid = threadIdx.x, lane = tid & 63, wave = __builtin_amdgcn_readfirstlane(tid >> 6), r = lane >> 4, c = lane & 15;
  gu64* PA = (gu64*)(ws + WS_PA);
  gu64* HG = (gu64*)(ws + WS_HG);
  gu64* PF = (gu64*)(ws + WS_PF);
  gu32* tmo = (gu32*)(ws + WS_TMO);
  STAMP(ws, 1, 0);
  {
    const int b = blockIdx.x, h = b >> 3, cs = b & 7;
    const float* rec = part + (size_t)h * NSPLIT * PREC;
    const int dd = tid & 127;
    float pm[NSPLIT], pl[NSPLIT], po[NSPLIT];
#pragma unroll
    for (int sp = 0; sp < NSPLIT; ++sp) { pm[sp] = rec[sp * PREC + 128]; pl[sp] = rec[sp * PREC + 129]; po[sp] = rec[sp * PREC + dd]; }
    u32x4 wov[4];
#pragma unroll
    for (int i = 0; i < 4; ++i) wov[i] = __builtin_nontemporal_load(wo + ((size_t)(h * 128 + (wave * 4 + i) * 8 + (lane >> 3)) * 512 + cs * 64 + (lane & 7) * 8) / 8);
    float* oh = lds;
    {
      float M = pm[0];
#pragma unroll
      for (int sp = 1; sp < NSPLIT; ++sp) M = fmaxf(M, pm[sp]);
      float L = 0.f, o = 0.f;
#pragma unroll
      for (int sp = 0; sp < NSPLIT; ++sp) {
        const float e = __expf(pm[sp] - M);
        L = fmaf(e, pl[sp], L);
        o = fmaf(e, po[sp], o);
      }
      if (tid < 128) oh[tid] = bf16r(o / L);
    }
    __syncthreads();
    STAMP(ws, 1, 1);
    float acc[8];
#pragma unroll
    for (int j = 0; j < 8; ++j) acc[j] = 0.f;
#pragma unroll
    for (int i = 0; i < 4; ++i) {
      const float ov = oh[(wave * 4 + i) * 8 + (lane >> 3)];
#pragma unroll
      for (int k = 0; k < 4; ++k) { acc[2 * k] = fmaf(ov, bflo(wov[i][k]), acc[2 * k]); acc[2 * k + 1] = fmaf(ov, bfhi(wov[i][k]), acc[2 * k + 1]); }
    }
#pragma unroll
    for (int j = 0; j < 8; ++j) { acc[j] += dpp<ROR8>(acc[j]); acc[j] = xrow_sum(acc[j]); }
    float* red = lds + 128;
    if (lane < 8) {
      *(f32x4*)(red + wave * 64 + lane * 8) = f32x4{acc[0], acc[1], acc[2], acc[3]};
      *(f32x4*)(red + wave * 64 + lane * 8 + 4) = f32x4{acc[4], acc[5], acc[6], acc[7]};
    }
    __syncthreads();
    if (tid < 64) {
      const float v = (red[tid] + red[64 + tid]) + (red[128 + tid] + red[192 + tid]);
      store_granule(PA + h * 512 + cs * 64 + tid, v);
      STAMP(ws, 1, 2);
    }
  }
  if ((int)blockIdx.x >= FG + NRED) return;
  if ((int)blockIdx.x >= FG) {
    if (wave != 0) return;
    const int col = ((int)blockIdx.x - FG) * 16 + c;
    const float xcol = bf2f(x[col]), b2col = bf2f(b2[col]);
    float v[11];
    for (unsigned spins = 0;;) {
      bool ok = true;
#pragma unroll
      for (int k = 0; k < 8; ++k) {
        const u64 gv = __hip_atomic_load(PA + (r + 4 * k) * 512 + col, RLX_AGENT);
        v[k] = __uint_as_float((u32)gv); ok &= (gv >> 32) == 1ull;
      }
      if (__all(ok)) break;
      if (spin_fail(spins, tmo, lane)) break;
    }
    STAMP(ws, 1, 3);
    float a = 0.f;
#pragma unroll
    for (int k = 0; k < 8; ++k) a += v[k];
    a = xrow_sum(a);
    const float hval = bf16r(xcol + bf16r(a));
    if (lane < 16) store_granule(HG + col, hval);
    for (unsigned spins = 0;;) {
      bool ok = true;
#pragma unroll
      for (int k = 0; k < 11; ++k) {
        const int gg = r + 4 * k;
        if (gg < FG) {
          const u64 gv = __hip_atomic_load(PF + gg * 512 + col, RLX_AGENT);
          v[k] = __uint_as_float((u32)gv); ok &= (gv >> 32) == 1ull;
        } else v[k] = 0.f;
      }
      if (__all(ok)) break;
      if (spin_fail(spins, tmo, lane)) break;
    }
    STAMP(ws, 1, 6);
    a = 0.f;
#pragma unroll
    for (int k = 0; k < 11; ++k) a += v[k];
    a = xrow_sum(a);
    const float o = bf16r(hval + bf16r(bf16r(a) + b2col));
    if (lane < 16) out[col] = f2bf_bits(o);
    STAMP(ws, 1, 7);
  } else {
    const int g = blockIdx.x;
    u32x4 w1v[8], w2v[8];
#pragma unroll
    for (int i = 0; i < 8; ++i) w1v[i] = __builtin_nontemporal_load(w1 + ((size_t)((wave * 8 + i) * 16 + c) * F + g * FC + r * 8) / 8);
#pragma unroll
    for (int i = 0; i < 8; ++i) w2v[i] = __builtin_nontemporal_load(w2 + ((size_t)(g * FC + wave * 8 + i) * 512 + lane * 8) / 8);
    const float b1v = bf2f(b1[g * FC + (lane & 31)]);
    float v[8];
    for (unsigned spins = 0;;) {
      bool ok = true;
#pragma unroll
      for (int k = 0; k < 8; ++k) {
        const u64 gv = __hip_atomic_load(HG + k * 64 + lane, RLX_AGENT);
        v[k] = __uint_as_float((u32)gv); ok &= (gv >> 32) == 1ull;
      }
      if (__all(ok)) break;
      if (spin_fail(spins, tmo, lane)) break;
    }
    STAMP(ws, 1, 4);
    float s1 = 0.f;
#pragma unroll
    for (int k = 0; k < 8; ++k) s1 += v[k];
    const float mean = wave_sum(s1) * (1.f / 512.f);
    float d2 = 0.f;
#pragma unroll
    for (int k = 0; k < 8; ++k) { const float d = v[k] - mean; d2 += d * d; }
    const float var = wave_sum(d2) * (1.f / 512.f);
    const float mb = bf16r(mean), den = bf16r(sqrtf(bf16r(bf16r(var) + 1e-5f)));
    float* hn = lds + wave * 512;
#pragma unroll
    for (int k = 0; k < 8; ++k) hn[k * 64 + lane] = bf16r(bf16r(v[k] - mb) / den);
    float acc[8];
#pragma unroll
    for (int j = 0; j < 8; ++j) acc[j] = 0.f;
#pragma unroll
    for (int i = 0; i < 8; ++i) {
      const float hv = hn[(wave * 8 + i) * 16 + c];
#pragma unroll
      for (int k = 0; k < 4; ++k) { acc[2 * k] = fmaf(hv, bflo(w1v[i][k]), acc[2 * k]); acc[2 * k + 1] = fmaf(hv, bfhi(w1v[i][k]), acc[2 * k + 1]); }
    }
#pragma unroll
    for (int j = 0; j < 8; ++j) acc[j] = row16_sum(acc[j]);
    float* red = lds + 4 * 512 + 4 * 512;
    if (c == 0) {
      *(f32x4*)(red + wave * 32 + r * 8) = f32x4{acc[0], acc[1], acc[2], acc[3]};
      *(f32x4*)(red + wave * 32 + r * 8 + 4) = f32x4{acc[4], acc[5], acc[6], acc[7]};
    }
    __syncthreads();
    STAMP(ws, 1, 9);
    const int f = lane & 31;
    const float t = bf16r((red[f] + red[32 + f]) + (red[64 + f] + red[96 + f]));
    const float h1 = bf16r(t + b1v);
    const float rr = fmaxf(h1, 0.f);
    const float act = bf16r(rr * rr);
#pragma unroll
    for (int j = 0; j < 8; ++j) acc[j] = 0.f;
#pragma unroll
    for (int i = 0; i < 8; ++i) {
      const float av = __builtin_bit_cast(float, __builtin_amdgcn_readlane(__builtin_bit_cast(int, act), wave * 8 + i));
#pragma unroll
      for (int k = 0; k < 4; ++k) { acc[2 * k] = fmaf(av, bflo(w2v[i][k]), acc[2 * k]); acc[2 * k + 1] = fmaf(av, bfhi(w2v[i][k]), acc[2 * k + 1]); }
    }
    float* red2 = lds + 4 * 512;
    *(f32x4*)(red2 + wave * 512 + lane * 8) = f32x4{acc[0], acc[1], acc[2], acc[3]};
    *(f32x4*)(red2 + wave * 512 + lane * 8 + 4) = f32x4{acc[4], acc[5], acc[6], acc[7]};
    __syncthreads();
    STAMP(ws, 1, 10);
#pragma unroll
    for (int u = 0; u < 2; ++u) {
      const int col = u * 256 + tid;
      const float vv = (red2[col] + red2[512 + col]) + (red2[1024 + col] + red2[1536 + col]);
      store_granule(PF + g * 512 + col, vv);
    }
    STAMP(ws, 1, 5);
  }
}

extern "C" void kernel_launch(void* const* d_in, const int* in_sizes, int n_in,
                              void* d_out, int out_size, void* d_ws, size_t ws_size,
                              hipStream_t stream) {
  const u32x4* x = (const u32x4*)d_in[0];
  const u32x4* kc = (const u32x4*)d_in[1];
  const u32x4* vc = (const u32x4*)d_in[2];
  const u32x4* wq = (const u32x4*)d_in[3];
  const u32x4* wo = (const u32x4*)d_in[4];
  const u32x4* w1 = (const u32x4*)d_in[6];
  const u16* b1 = (const u16*)d_in[7];
  const u32x4* w2 = (const u32x4*)d_in[8];
  const u16* b2 = (const u16*)d_in[9];
  unsigned char* ws = (unsigned char*)d_ws;
  hipLaunchKernelGGL(k1_attn, dim3(256), dim3(1024), 0, stream, x, kc, vc, wq, (float*)(ws + WS_PART), (u32x4*)(ws + WS_PA));
  hipLaunchKernelGGL(k2_tail, dim3(K2_GRID), dim3(256), 0, stream, (const u16*)x, (const float*)(ws + WS_PART), wo, w1, b1, w2, b2,
                     (u16*)d_out, ws);
}
```
